```python
import torch

def _softmax(x, axis=-1):
    return torch.softmax(x, dim=axis)
def _gelu(x):
    return torch.nn.functional.gelu(x, approximate="tanh")
def _silu(x):
    return torch.nn.functional.silu(x)
def _sigmoid(x):
    return torch.sigmoid(x)
def _layernorm(x, axis=-1, eps=1e-5):
    m = x.mean(dim=axis, keepdim=True)
    v = x.var(dim=axis, keepdim=True, unbiased=False)
    return (x - m) / torch.sqrt(v + eps)
def _scatter_add(tgt, idx, src, axis=0):
    return tgt.clone().index_add_(axis, idx, src)
def _segment_sum(src, seg, n):
    out = torch.zeros((n,) + src.shape[1:], dtype=src.dtype)
    return out.index_add_(0, seg, src)
def _pad(x, pw):
    flat = tuple(v for pair in reversed(pw) for v in pair)
    return torch.nn.functional.pad(x, flat)

kernel_name = 'sequential_preLN__sliding_local_dilated__swiglu_ffn__prompt'

def setup_inputs():

    _w = (lambda a: (lambda t: t.to(torch.bfloat16) if t.is_floating_point() else t)(torch.as_tensor(a)))
    def _A_setup():
        B, S, D = 1, 2048, 640
        return {
            "x": _w((lambda *s: torch.randn(*s, dtype=torch.bfloat16) * 0.05)(B, S, D)),
            "wqkv": _w((lambda *s: torch.randn(*s, dtype=torch.bfloat16) * 0.05)(D, 3 * D)),
            "wo": _w((lambda *s: torch.randn(*s, dtype=torch.bfloat16) * 0.05)(D, D)),
        }
    _A_in = _A_setup()

    def _B_setup():
        B, S, D, F = 1, 2048, 640, 1728
        return {
            "x": _w((lambda *s: torch.randn(*s, dtype=torch.bfloat16) * 0.05)(B, S, D)),
            "w_gate": _w((lambda *s: torch.randn(*s, dtype=torch.bfloat16) * 0.05)(D, F)),
            "w_up": _w((lambda *s: torch.randn(*s, dtype=torch.bfloat16) * 0.05)(D, F)),
            "w_down": _w((lambda *s: torch.randn(*s, dtype=torch.bfloat16) * 0.05)(F, D)),
        }
    _B_in = _B_setup()

    return {
        "A_x": _A_in["x"],
        "A_wqkv": _A_in["wqkv"],
        "A_wo": _A_in["wo"],
        "B_x": _B_in["x"],
        "B_w_gate": _B_in["w_gate"],
        "B_w_up": _B_in["w_up"],
        "B_w_down": _B_in["w_down"],
    }

def reference(A_x, A_wqkv, A_wo, B_x, B_w_gate, B_w_up, B_w_down):
    def _A_ref(x, wqkv, wo):
        B, S, D = x.shape
        H = 32; Dh = D // H
        W, dil = 8, 2
        qkv = (lambda x, *d: x.permute(*d))((x @ wqkv).reshape(B, S, 3, H, Dh), 2, 0, 3, 1, 4)
        q, k, v = qkv[0], qkv[1], qkv[2]
        scale = Dh ** -0.5
        pos = torch.arange(S)
        off = torch.arange(W) * dil
        idx = pos[:, None] - off[None, :]
        mask = idx >= 0
        idx_c = torch.where(mask, idx, 0).reshape(-1)
        kg = k[:, :, idx_c, :].reshape(B, H, S, W, Dh)
        vg = v[:, :, idx_c, :].reshape(B, H, S, W, Dh)
        logits = torch.einsum("bhsd,bhswd->bhsw", q, kg) * scale
        logits = torch.where(mask[None, None, :, :], logits, -1e9)
        attn = _softmax(logits, axis=-1)
        o = torch.einsum("bhsw,bhswd->bhsd", attn, vg)
        return (lambda x, *d: x.permute(*d))(o, 0, 2, 1, 3).reshape(B, S, D) @ wo

    def _B_ref(x, w_gate, w_up, w_down):
        g = _silu(x @ w_gate)
        u = x @ w_up
        return (g * u) @ w_down

    x = A_x
    h = x + _A_ref(_layernorm(x, axis=-1), A_wqkv, A_wo)
    h2 = h + _B_ref(_layernorm(h, axis=-1), B_w_gate, B_w_up, B_w_down)
    return h2
```

```cpp
#include <hip/hip_runtime.h>
#include <hip/hip_bf16.h>

typedef __attribute__((ext_vector_type(8))) __bf16 bf16x8;
typedef __attribute__((ext_vector_type(2))) __bf16 bf16x2;
typedef __attribute__((ext_vector_type(4))) short s16x4;
typedef __attribute__((ext_vector_type(4))) float f32x4;
typedef __attribute__((ext_vector_type(2))) float f32x2;
typedef __attribute__((ext_vector_type(4))) unsigned u32x4;
typedef __attribute__((ext_vector_type(2))) unsigned u32x2;
typedef __attribute__((address_space(1))) unsigned gu32;
#define LAS __attribute__((address_space(3)))
#define DEV __device__ __forceinline__
#define SCHED() __builtin_amdgcn_sched_barrier(0)

constexpr int S_ = 2048, D_ = 640, F_ = 1728;
constexpr unsigned WS_FLAG = 0;
constexpr unsigned WS_WO = 16384;
constexpr unsigned WS_WG = WS_WO + 800 * 1024;
constexpr unsigned WS_WU = WS_WG + 2160 * 1024;
constexpr unsigned WS_WD = WS_WU + 2160 * 1024;
constexpr unsigned WS_O = WS_WD + 2160 * 1024;
constexpr unsigned WS_H = WS_O + 2560 * 1024;
constexpr unsigned WS_A = WS_H + 2560 * 1024;
constexpr unsigned WS_END = WS_A + 6912 * 1024;

#define ST_DECL
#define STAMP(i)
#define ST_OUT(k, n)
#define WAIT_V(n) asm volatile("s_waitcnt vmcnt(%0)" ::"n"(n) : "memory")
#define WAIT_L0() asm volatile("s_waitcnt lgkmcnt(0)" ::: "memory")
DEV void bar() { SCHED(); asm volatile("s_waitcnt lgkmcnt(0)\n\ts_barrier" ::: "memory"); SCHED(); }

DEV void bar_raw() { SCHED(); asm volatile("s_barrier" ::: "memory"); SCHED(); }
DEV u32x4 mk_srd(const void* p, unsigned bytes) {
  unsigned long long a = (unsigned long long)p;
  u32x4 r = {(unsigned)a, (unsigned)(a >> 32) & 0xffffu, bytes, 0x00020000u};
  return r;
}
DEV __amdgpu_buffer_rsrc_t mk_rsrc(const void* p, unsigned bytes) {
  return __builtin_amdgcn_make_buffer_rsrc((void*)p, 0, bytes, 0x00020000);
}
DEV void dma16(unsigned ldsdst, unsigned voff, u32x4 srd, unsigned soff) {
  asm volatile("s_mov_b32 m0, %0\n\ts_nop 0\n\tbuffer_load_dwordx4 %1, %2, %3 offen lds" ::"s"(ldsdst), "v"(voff), "s"(srd), "s"(soff) : "memory");
}
DEV void dma16_sc1(unsigned ldsdst, unsigned voff, u32x4 srd, unsigned soff) {
  asm volatile("s_mov_b32 m0, %0\n\ts_nop 0\n\tbuffer_load_dwordx4 %1, %2, %3 offen sc1 lds" ::"s"(ldsdst), "v"(voff), "s"(srd), "s"(soff) : "memory");
}
DEV f32x4 mfma(u32x4 a, u32x4 b, f32x4 c) {
  return __builtin_amdgcn_mfma_f32_16x16x32_bf16(__builtin_bit_cast(bf16x8, a), __builtin_bit_cast(bf16x8, b), c, 0, 0, 0);
}
DEV unsigned pk(float lo, float hi) {
  f32x2 v = {lo, hi};
  return __builtin_bit_cast(unsigned, __builtin_convertvector(v, bf16x2));
}
DEV float dot2(unsigned a, unsigned b, float acc) {
  return __builtin_amdgcn_fdot2_f32_bf16(__builtin_bit_cast(bf16x2, a), __builtin_bit_cast(bf16x2, b), acc, false);
}
DEV float bflo(unsigned v) { return __uint_as_float(v << 16); }
DEV float bfhi(unsigned v) { return __uint_as_float(v & 0xffff0000u); }
DEV u32x4 ldsr128(LAS char* l, unsigned off) { return *(LAS u32x4*)(l + off); }
DEV u32x2 ldsr64(LAS char* l, unsigned off) { return *(LAS u32x2*)(l + off); }
DEV u32x2 ldstr(LAS char* l, unsigned off) {
  return __builtin_bit_cast(u32x2, __builtin_amdgcn_ds_read_tr16_b64_v4i16((LAS s16x4*)(l + off)));
}
DEV float sel4(f32x4 v, int i) { float a = (i & 1) ? v[1] : v[0], b = (i & 1) ? v[3] : v[2]; return (i & 2) ? b : a; }
constexpr unsigned ONES = 0x3f803f80u;

DEV unsigned wrap(unsigned k, unsigned n) {
  unsigned r;
  asm("s_cmp_ge_u32 %1, %2\n\ts_cselect_b32 %0, %2, 0\n\ts_sub_u32 %0, %1, %0" : "=&s"(r) : "s"(k), "s"(n) : "scc");
  return r;
}
DEV void map_block(int b, int& g, int& m) {
  int x = b & 7, i = b >> 3;
  g = (x >> 1) * 4 + (i >> 3);
  m = (x & 1) * 8 + (i & 7);
}

constexpr unsigned K1_STG = 34816, K1_BOFF = 18432;
constexpr unsigned K1_QKV = 0, K1_QSTR = 304, K1_OST = 45056, K1_STAT = 57344;
constexpr unsigned K1_LDS = 4 * K1_STG;

template <int RG>
DEV void k1_gemm(LAS char* lds, unsigned ldsb, int w, int lane, u32x4 srdX, u32x4 srdW, unsigned vA0, unsigned vA1, unsigned vA2,
                 unsigned vB0, unsigned vB1, u32x4 srdP, unsigned voP, unsigned ldP, unsigned hi4, const unsigned (&dst)[4], __amdgpu_buffer_rsrc_t rws, unsigned rot) {
  constexpr int NR = RG == 0 ? 5 : 4, R0 = RG == 0 ? 0 : 5, NX = RG == 0 ? 5 : 6;
  const int cgp = w & 3, j = lane & 15, q = lane >> 4;
  f32x4 acc[NR][2], sq[2];
#pragma unroll
  for (int r = 0; r < NR; ++r) { acc[r][0] = f32x4{0, 0, 0, 0}; acc[r][1] = f32x4{0, 0, 0, 0}; }
  sq[0] = sq[1] = f32x4{0, 0, 0, 0};
  const unsigned aA[2] = {(unsigned)(j * 128 + ((q ^ (j & 7)) << 4)), (unsigned)(j * 128 + (((4 + q) ^ (j & 7)) << 4))};
  unsigned aB[2][2];
#pragma unroll
  for (int c = 0; c < 2; ++c)
#pragma unroll
    for (int h = 0; h < 2; ++h) {
      int row = 8 * q + 4 * h + (j >> 2);
      int f = ((row & 3) << 2) | ((row >> 2) & 3);
      aB[c][h] = K1_BOFF + row * 256 + (((2 * (2 * cgp + c) + ((j & 3) >> 1)) ^ f) << 4) + 8 * (j & 1);
    }
  const bool onesW = (cgp == 3) && (j >= 8), onesX = (j == 0);
  u32x4 wf[2][2], xf[2][NX];
  auto rd = [&](int it, int s) {
    const unsigned so = (it & 3) * K1_STG;
#pragma unroll
    for (int c = 0; c < 2; ++c) {
      u32x2 lo = ldstr(lds, so + s * 8192 + aB[c][0]), hi = ldstr(lds, so + s * 8192 + aB[c][1]);
      wf[s][c] = u32x4{lo[0], lo[1], hi[0], hi[1]};
    }
#pragma unroll
    for (int r = 0; r < NR; ++r) xf[s][r] = ldsr128(lds, so + (R0 + r) * 2048 + aA[s]);
    if (RG == 1) { xf[s][4] = ldsr128(lds, so + (5 + cgp) * 2048 + aA[s]); xf[s][5] = ldsr128(lds, so + (1 + cgp) * 2048 + aA[s]); }
    SCHED();
  };
  auto mm = [&](int s) {
    if (onesW) wf[s][1] = u32x4{ONES, ONES, ONES, ONES};
    if (RG == 0) { if (onesX) xf[s][0] = u32x4{ONES, ONES, ONES, ONES}; }
#pragma unroll
    for (int r = 0; r < NR; ++r) { acc[r][0] = mfma(wf[s][0], xf[s][r], acc[r][0]); acc[r][1] = mfma(wf[s][1], xf[s][r], acc[r][1]); }
    if (RG == 1) { sq[0] = mfma(xf[s][4], xf[s][4], sq[0]); sq[1] = mfma(xf[s][5], xf[s][5], sq[1]); }
    else sq[0] = mfma(xf[s][0], xf[s][0], sq[0]);
    SCHED();
  };
  u32x2 p[8];
  auto stage = [&](int sg) {
    if (sg < 10) {
      const unsigned sb = ldsb + (sg & 3) * K1_STG, kk = wrap(sg + rot, 10);
      dma16(sb + (2 + 2 * w) * 1024, vA0, srdX, kk * 128);
      dma16(sb + (3 + 2 * w) * 1024, vA1, srdX, kk * 128);
      if (RG == 0 && w < 2) dma16(sb + w * 1024, vA2, srdX, kk * 128);
      dma16(sb + K1_BOFF + (2 * w) * 1024, vB0, srdW, kk * (64 * 3840));
      dma16(sb + K1_BOFF + (2 * w + 1) * 1024, vB1, srdW, kk * (64 * 3840));
      SCHED();
    }
  };
  auto fill = [&](int it) {
    if (it == 0) stage(3);
    stage(it + 4);
    if (it == 1) {
      asm volatile("s_nop 4" ::: "memory");
#pragma unroll
      for (int e = 0; e < 8; ++e) {
        unsigned so = (e & 3) * ldP + (e >> 2) * hi4;
        asm volatile("buffer_load_dwordx2 %0, %1, %2, %3 offen" : "=v"(p[e]) : "v"(voP), "s"(srdP), "s"(so) : "memory");
      }
      SCHED();
    }
  };
  if (RG == 0 && w < 2) WAIT_V(5); else WAIT_V(4);
  bar_raw();
  rd(0, 0);
  stage(2);
#pragma unroll
  for (int it = 0; it < 10; ++it) {
    rd(it, 1);
    mm(0);
    if (it < 9) {
      if (RG == 0 && w < 2) { if (it == 0) WAIT_V(5); else if (it >= 2 && it < 5) WAIT_V(18); else if (it < 7) WAIT_V(10); else if (it == 7) WAIT_V(9); else WAIT_V(4); }
      else { if (it == 0) WAIT_V(4); else if (it >= 2 && it < 5) WAIT_V(16); else if (it < 8) WAIT_V(8); else WAIT_V(4); }
      bar();
      if (it == 6) {
        asm volatile("" : "+v"(p[0]), "+v"(p[1]), "+v"(p[2]), "+v"(p[3]), "+v"(p[4]), "+v"(p[5]), "+v"(p[6]), "+v"(p[7])::"memory");
        const unsigned base = 2 * K1_STG + w * 4096;
#pragma unroll
        for (int f = 0; f < 4; ++f) {
          u32x4 v;
#pragma unroll
          for (int d = 0; d < 4; ++d)
            v[d] = __builtin_amdgcn_perm(p[2 * d + 1][f >> 1], p[2 * d][f >> 1], (f & 1) ? 0x07060302u : 0x05040100u);
          *(LAS u32x4*)(lds + base + (j >> 2) * 1024 + q * 256 + (4 * (j & 3) + f) * 16) = v;
        }
        WAIT_L0();
#pragma unroll
        for (int f = 0; f < 4; ++f) {
          u32x4 v = ldsr128(lds, base + f * 1024 + lane * 16);
          __builtin_amdgcn_raw_buffer_store_b128(v, rws, lane * 16 + dst[f], 0, 16);
        }
        asm volatile("" ::: "memory");
      }
      rd(it + 1, 0);
    }
    if (RG == 0) fill(it);
    mm(1);
    if (RG == 1) fill(it);
  }
  bar();
  LAS float* st = (LAS float*)(lds + K1_STAT);
  if (cgp == 3 && q == 2) {
#pragma unroll
    for (int r = 0; r < NR; ++r) st[(R0 + r) * 16 + j] = acc[r][1][0];
  }
  if (RG == 0) {
    if (j == 0) {
      *(LAS f32x4*)(st + 288 + (2 * cgp) * 16 + 4 * q) = acc[0][0];
      *(LAS f32x4*)(st + 288 + (2 * cgp + 1) * 16 + 4 * q) = acc[0][1];
    }
    if (cgp == 0 && q == (j >> 2)) st[144 + j] = sel4(sq[0], j & 3);
  } else if (q == (j >> 2)) {
    st[144 + (5 + cgp) * 16 + j] = sel4(sq[0], j & 3);
    st[144 + (1 + cgp) * 16 + j] = sel4(sq[1], j & 3);
  }
  bar();
  f32x4 cs[2];
  unsigned co[2];
#pragma unroll
  for (int c = 0; c < 2; ++c) {
    cs[c] = *(LAS f32x4*)(st + 288 + (2 * cgp + c) * 16 + 4 * q);
    int ci = 4 * (2 * cgp + c) + q;
    co[c] = (ci / 5) * 48 + (ci % 5) * 8;
  }
#pragma unroll
  for (int r = 0; r < NR; ++r) {
    int row = (R0 + r) * 16 + j;
    float mean = st[row] * (1.0f / D_);
    float var = fmaxf(st[144 + row] * (1.0f / D_) - mean * mean, 0.0f);
    float rstd = __builtin_amdgcn_rsqf(var + 1e-5f);
#pragma unroll
    for (int c = 0; c < 2; ++c) {
      f32x4 v = (acc[r][c] - mean * cs[c]) * rstd;
      *(LAS u32x2*)(lds + K1_QKV + row * K1_QSTR + co[c]) = u32x2{pk(v[0], v[1]), pk(v[2], v[3])};
    }
  }
}

__global__ __launch_bounds__(512, 2) void k1_qkv_attn(const char* __restrict__ x, const char* __restrict__ wqkv, const char* __restrict__ wo,
                                                      const char* __restrict__ wg, const char* __restrict__ wu, const char* __restrict__ wd,
                                                      char* __restrict__ ws) {
  __shared__ __attribute__((aligned(1024))) char lds_[K1_LDS];
  LAS char* lds = (LAS char*)lds_;
  const unsigned ldsb = (unsigned)(unsigned long long)lds;
  const int tid = threadIdx.x, lane = tid & 63, w = __builtin_amdgcn_readfirstlane(tid >> 6);
  const int j = lane & 15, q = lane >> 4;
  int g, hp;
  ST_DECL; STAMP(0);
  map_block(blockIdx.x, g, hp);
  const unsigned rot = wrap(((hp & 3) + (g & 3)) * 2 + ((blockIdx.x >> 1) & 1), 10);

  const u32x4 srdX = mk_srd(x, S_ * 1280), srdW = mk_srd(wqkv, 640 * 3840);
  unsigned vA0, vA1, vA2, vB0, vB1;
  {
    int r8 = lane >> 3, ch = (lane & 7) ^ r8;
    int r0 = g * 128 - 16;
    vA0 = (r0 + 16 + 16 * w + r8) * 1280 + ch * 16;
    vA1 = vA0 + 8 * 1280;
    int rh = r0 + 8 * (w & 1) + r8;
    vA2 = (rh < 0 ? 0 : rh) * 1280 + ch * 16;
#pragma unroll
    for (int i = 0; i < 2; ++i) {
      int row = 8 * w + 4 * i + q;
      int c = j ^ (((row & 3) << 2) | ((row >> 2) & 3));
      if (c == 15) c = 14;
      unsigned v = row * 3840 + (c / 5) * 1280 + hp * 80 + (c % 5) * 16;
      if (i == 0) vB0 = v; else vB1 = v;
    }
  }
  asm volatile("s_nop 4" ::: "memory");
#pragma unroll
  for (int it = 0; it < 2; ++it) {
    const unsigned sb = ldsb + it * K1_STG, kk = wrap(it + rot, 10);
    dma16(sb + (2 + 2 * w) * 1024, vA0, srdX, kk * 128);
    dma16(sb + (3 + 2 * w) * 1024, vA1, srdX, kk * 128);
    if (w < 2) dma16(sb + w * 1024, vA2, srdX, kk * 128);
    dma16(sb + K1_BOFF + (2 * w) * 1024, vB0, srdW, kk * (64 * 3840));
    dma16(sb + K1_BOFF + (2 * w + 1) * 1024, vB1, srdW, kk * (64 * 3840));
  }
  int blk = (blockIdx.x * 8 + w) % 1820;
  const char* wsrc; int ld, ks, cb; unsigned wbytes; bool isd = false; unsigned dst[4];
  if (blk < 200) { wsrc = wo; ld = 1280; ks = blk / 10; cb = blk % 10; wbytes = 640 * 1280;
    for (int f = 0; f < 4; ++f) { int c = 4 * cb + f; dst[f] = WS_WO + (((c / 5) * 20 + ks) * 5 + c % 5) * 1024; } }
  else if (blk < 1280) { int b2 = blk - 200; bool up = b2 >= 540; if (up) b2 -= 540; wsrc = up ? wu : wg; ld = 3456; ks = b2 / 27; cb = b2 % 27; wbytes = 640 * 3456;
    for (int f = 0; f < 4; ++f) dst[f] = (up ? WS_WU : WS_WG) + ((4 * cb + f) * 20 + ks) * 1024; }
  else { int b2 = blk - 1280; wsrc = wd; ld = 1280; ks = b2 / 10; cb = b2 % 10; wbytes = 1728 * 1280; isd = true;
    for (int f = 0; f < 4; ++f) { int c = 4 * cb + f; dst[f] = WS_WD + (((c / 5) * 54 + ks) * 5 + c % 5) * 1024; } }
  const u32x4 srdP = mk_srd(wsrc, wbytes);
  const unsigned voP = (32 * ks + (isd ? 4 : 8) * q) * ld + cb * 128 + j * 8, hi4 = (isd ? 16 : 4) * ld;
  const __amdgpu_buffer_rsrc_t rws = mk_rsrc(ws, WS_END);
  STAMP(1);
  if (w < 4) k1_gemm<0>(lds, ldsb, w, lane, srdX, srdW, vA0, vA1, vA2, vB0, vB1, srdP, voP, ld, hi4, dst, rws, rot);
  else k1_gemm<1>(lds, ldsb, w, lane, srdX, srdW, vA0, vA1, vA2, vB0, vB1, srdP, voP, ld, hi4, dst, rws, rot);
  if (blockIdx.x == 0 && tid < 48) __builtin_amdgcn_raw_buffer_store_b32(0u, rws, WS_FLAG + tid * 256, 0, 16);
  bar();
  STAMP(2);
  {
    const int hh = w & 1, r = 32 * (w >> 1) + (lane & 31), wh = lane >> 5;
    const unsigned qa = K1_QKV + (r + 16) * K1_QSTR + hh * 48;
    u32x4 q0 = ldsr128(lds, qa), q1 = ldsr128(lds, qa + 16);
    u32x2 q2 = ldsr64(lds, qa + 32);
    const unsigned ka = qa + 96 - 8 * wh * K1_QSTR;
    float s[4];
    unsigned v[4][10];
#pragma unroll
    for (int t = 0; t < 4; ++t) {
      const unsigned a = ka - 2 * t * K1_QSTR;
      u32x4 k0 = ldsr128(lds, a), k1 = ldsr128(lds, a + 16);
      u32x2 k2 = ldsr64(lds, a + 32);
      u32x4 v0 = ldsr128(lds, a + 96), v1 = ldsr128(lds, a + 112);
      u32x2 v2 = ldsr64(lds, a + 128);
      float d0 = 0.f, d1 = 0.f;
#pragma unroll
      for (int i = 0; i < 4; ++i) { d0 = dot2(q0[i], k0[i], d0); d1 = dot2(q1[i], k1[i], d1); }
      d0 = dot2(q2[0], k2[0], d0); d1 = dot2(q2[1], k2[1], d1);
      const bool ok = (g * 128 + r - 2 * (4 * wh + t)) >= 0;
      s[t] = ok ? (d0 + d1) * 0.22360679774997896f : -1e9f;
#pragma unroll
      for (int i = 0; i < 4; ++i) { v[t][i] = v0[i]; v[t][4 + i] = v1[i]; }
      v[t][8] = v2[0]; v[t][9] = v2[1];
    }
    float mx = fmaxf(fmaxf(s[0], s[1]), fmaxf(s[2], s[3]));
    { auto rr = __builtin_amdgcn_permlane32_swap(__float_as_uint(mx), __float_as_uint(mx), false, false);
      mx = fmaxf(__uint_as_float(rr[0]), __uint_as_float(rr[1])); }
    float e[4], sum = 0.f;
#pragma unroll
    for (int t = 0; t < 4; ++t) { e[t] = __builtin_amdgcn_exp2f((s[t] - mx) * 1.4426950408889634f); sum += e[t]; }
    { auto rr = __builtin_amdgcn_permlane32_swap(__float_as_uint(sum), __float_as_uint(sum), false, false);
      sum = __uint_as_float(rr[0]) + __uint_as_float(rr[1]); }
    const float inv = __builtin_amdgcn_rcpf(sum);
    const unsigned p01 = pk(e[0] * inv, e[1] * inv), p23 = pk(e[2] * inv, e[3] * inv);
    float o[20];
#pragma unroll
    for (int i = 0; i < 10; ++i) {
      o[2 * i] = dot2(__builtin_amdgcn_perm(v[3][i], v[2][i], 0x05040100u), p23, dot2(__builtin_amdgcn_perm(v[1][i], v[0][i], 0x05040100u), p01, 0.f));
      o[2 * i + 1] = dot2(__builtin_amdgcn_perm(v[3][i], v[2][i], 0x07060302u), p23, dot2(__builtin_amdgcn_perm(v[1][i], v[0][i], 0x07060302u), p01, 0.f));
    }
    float f[10];
#pragma unroll
    for (int d = 0; d < 10; ++d) {
      auto rr = __builtin_amdgcn_permlane32_swap(__float_as_uint(o[d]), __float_as_uint(o[d + 10]), false, false);
      f[d] = __uint_as_float(rr[0]) + __uint_as_float(rr[1]);
    }
    LAS unsigned* op = (LAS unsigned*)(lds + K1_OST + r * 80 + hh * 40 + wh * 20);
#pragma unroll
    for (int i = 0; i < 5; ++i) op[i] = pk(f[2 * i], f[2 * i + 1]);
  }
  bar();
  STAMP(3);
#pragma unroll
  for (int i = 0; i < 2; ++i) {
    const int T = tid + 512 * i;
    if (T < 640) {
      const int oct = T >> 7, row = T & 127, col0 = 40 * hp + 8 * oct;
      u32x4 v = ldsr128(lds, K1_OST + row * 80 + oct * 16);
      const unsigned off = ((g * 8 + (row >> 4)) * 20 + (col0 >> 5)) * 1024 + ((col0 & 31) >> 3) * 256 + (row & 15) * 16;
      __builtin_amdgcn_raw_buffer_store_b128(v, rws, off + WS_O, 0, 16);
    }
  }
}

constexpr unsigned K2_PART = 102400, K2_PSTR = 336, K2_HST = 147456, K2_STAT = 157696, K2_LDS = 159744;
constexpr int NEARLY = 8;

DEV void wait_flag(gu32* f) {
#pragma nounroll
  for (unsigned spins = 0; spins < (1u << 22); ++spins) {
    if ((unsigned)__builtin_amdgcn_readfirstlane(__hip_atomic_load(f, __ATOMIC_RELAXED, __HIP_MEMORY_SCOPE_AGENT)) >= 16u) break;
    __builtin_amdgcn_s_sleep(1);
  }
  asm volatile("" ::: "memory");
}
DEV void signal_flag(gu32* f, int lane) {
  if (lane == 0) __hip_atomic_fetch_add(f, 1u, __ATOMIC_RELAXED, __HIP_MEMORY_SCOPE_AGENT);
}
DEV unsigned a3_off(int H, int ks) { return H == 0 ? ks * 4096 : (ks < 16 ? 81920 + ks * 4096 : (ks - 16) * 4096); }

template <bool STATS>
DEV void k2_body(LAS char* lds, unsigned ldsb, int w, int tid, int g, int m, const char* x, char* ws, char* out) {
  const int lane = tid & 63, j = lane & 15, q = lane >> 4;
  const int cg = m & 7, s = m >> 3;
  const unsigned v16 = lane * 16;
  const u32x4 srdWS = mk_srd(ws, WS_END);
  const __amdgpu_buffer_rsrc_t rws = mk_rsrc(ws, WS_END), rx = mk_rsrc(x, S_ * 1280), rout = mk_rsrc(out, S_ * 1280);
  gu32* flags = (gu32*)(ws + WS_FLAG + g * 768);
  const u32x4 ones = {ONES, ONES, ONES, ONES};
  const int nf = m < 12 ? 7 : 6, ff = (m < 12 ? 7 * m : 84 + 6 * (m - 12)) + w;
  const bool act = w < nf;
  const unsigned vB3 = act ? v16 : 0x7fffff00u;
  ST_DECL; STAMP(0);
  asm volatile("s_nop 4" ::: "memory");
#pragma unroll
  for (int i = 0; i < 13; ++i) {
    int fr = i * 8 + w; if (i == 12) fr = 96 + (w & 3);
    dma16(ldsb + fr * 1024, v16, srdWS, WS_WO + cg * 102400 + fr * 1024);
  }
  const int ri = w & 3, kh = w >> 2;
  u32x4 a2[10];
  {
    const unsigned so = WS_O + ((g * 8 + (ri >> 1) * 4 + 2 * s + (ri & 1)) * 20 + kh * 10) * 1024;
#pragma unroll
    for (int i = 0; i < 10; ++i) a2[i] = __builtin_amdgcn_raw_buffer_load_b128(rws, v16 + (i & 3) * 1024, so + (i >> 2) * 4096, 0);
  }
  u32x4 xr[2];
  {
    const int grow = g * 128 + (lane >> 5) * 64 + s * 32 + (lane & 31);
    const unsigned vo = grow * 1280 + cg * 160;
    xr[0] = __builtin_amdgcn_raw_buffer_load_b128(rx, vo, w * 16, 0);
    xr[1] = __builtin_amdgcn_raw_buffer_load_b128(rx, vo, 128 + (w & 1) * 16, 0);
  }
  u32x4 bg[20], bu[20];
  constexpr unsigned kb[5] = {0, 4096, 8192, 12288, 16384};
  const unsigned sog = WS_WG + ff * 20480, sou = WS_WU + ff * 20480;
  if (STATS) __builtin_amdgcn_s_setprio(3);
  WAIT_V(12);
  bar();
  STAMP(1);
  {
    f32x4 acc[5];
#pragma unroll
    for (int c = 0; c < 5; ++c) acc[c] = f32x4{0, 0, 0, 0};
    u32x4 wf[2][5];
#pragma unroll
    for (int c = 0; c < 5; ++c) wf[0][c] = ldsr128(lds, kh * 51200 + c * 1024 + v16);
#pragma unroll
    for (int i = 0; i < 10; ++i) {
      if (i < 9) {
#pragma unroll
        for (int c = 0; c < 5; ++c) wf[(i + 1) & 1][c] = ldsr128(lds, kh * 51200 + ((i + 1) * 5 + c) * 1024 + v16);
      }
      SCHED();
#pragma unroll
      for (int c = 0; c < 5; ++c) acc[c] = mfma(wf[i & 1][c], a2[i], acc[c]);
      SCHED();
      if (!STATS) {
#pragma unroll
        for (int k = i - (10 - NEARLY); k >= 0 && k == i - (10 - NEARLY); ++k) {
          bg[k] = __builtin_amdgcn_raw_buffer_load_b128(rws, vB3 + (k & 3) * 1024, sog + kb[k >> 2], 0);
          bu[k] = __builtin_amdgcn_raw_buffer_load_b128(rws, vB3 + (k & 3) * 1024, sou + kb[k >> 2], 0);
        }
        SCHED();
      }
    }
#pragma unroll
    for (int c = 0; c < 5; ++c) *(LAS f32x4*)(lds + K2_PART + kh * 21504 + (16 * ri + j) * K2_PSTR + (16 * c + 4 * q) * 4) = acc[c];
  }
  bar();
#pragma unroll
  for (int i = 0; i < 2; ++i) {
    {
      const int oct = i == 0 ? w : 8 + (w & 1);
      const unsigned pa = K2_PART + lane * K2_PSTR + oct * 32;
      f32x4 lo = *(LAS f32x4*)(lds + pa) + *(LAS f32x4*)(lds + pa + 21504);
      f32x4 hi = *(LAS f32x4*)(lds + pa + 16) + *(LAS f32x4*)(lds + pa + 21504 + 16);
      u32x4 h = {pk(lo[0] + bflo(xr[i][0]), lo[1] + bfhi(xr[i][0])), pk(lo[2] + bflo(xr[i][1]), lo[3] + bfhi(xr[i][1])),
                 pk(hi[0] + bflo(xr[i][2]), hi[1] + bfhi(xr[i][2])), pk(hi[2] + bflo(xr[i][3]), hi[3] + bfhi(xr[i][3]))};
      *(LAS u32x4*)(lds + K2_HST + ((q * 10 + oct) * 16 + j) * 16) = h;
    }
  }
  bar();
  STAMP(2);
  u32x4 bd[35];
  const int t0 = w < 7 ? 7 * w : 48;
  const unsigned sod = WS_WD + (cg * 54 + t0) * 5120;
  const unsigned v7 = w < 6 ? v16 : 0x7fffff00u;
  f32x4 csg = {0, 0, 0, 0}, csu = {0, 0, 0, 0};
  if (STATS) {
#pragma unroll
    for (int i = 0; i < 10; ++i) {
      const int c4 = i * 4 + q, r4 = c4 / 10, oct = c4 % 10, col0 = 80 * cg + 8 * oct;
      u32x4 v = ldsr128(lds, K2_HST + i * 1024 + v16);
      const unsigned off = ((g * 8 + (r4 >> 1) * 4 + 2 * s + (r4 & 1)) * 20 + (col0 >> 5)) * 1024 + ((col0 & 31) >> 3) * 256 + j * 16;
      __builtin_amdgcn_raw_buffer_store_b128(v, rws, off + WS_H, 0, 16);
    }
    bar_raw();
    WAIT_V(0);
    signal_flag(flags, lane);
    wait_flag(flags);
#pragma unroll
    for (int i = 0; i < 16; ++i) dma16_sc1(ldsb + i * 1024, v16, srdWS, WS_H + (g * 8 + (i & 3)) * 20480 + (i >> 2) * 1024 + kb[0]);
  } else {
    bar_raw();
#pragma unroll
    for (int i = NEARLY; i < 20; ++i) {
      bg[i] = __builtin_amdgcn_raw_buffer_load_b128(rws, vB3 + (i & 3) * 1024, sog + kb[i >> 2], 0);
      bu[i] = __builtin_amdgcn_raw_buffer_load_b128(rws, vB3 + (i & 3) * 1024, sou + kb[i >> 2], 0);
    }
    SCHED();
#pragma unroll
    for (int i = 0; i < 20; ++i) { csg = mfma(bg[i], ones, csg); csu = mfma(bu[i], ones, csu); }
    asm volatile("" : "+v"(csg), "+v"(csu)::"memory");
  }
  bar();
  STAMP(5);
  const unsigned soh = WS_H + (g * 8 + (w & 3)) * 20480 + (w >> 2) * 1024;
#pragma unroll
  for (int i = 2; i < 5; ++i) dma16_sc1(ldsb + (i * 8 + w) * 1024, v16, srdWS, soh + kb[i >> 1] + (i & 1) * 2048);
  LAS f32x2* st = (LAS f32x2*)(lds + K2_STAT);
  u32x4 L[14];
  const unsigned soa = WS_A + ((g * 4 + s) * 108 + 2 * t0) * 1024;
#pragma unroll
  for (int H = 0; H < 2; ++H) {
    f32x4 ag[4], au[4];
    u32x4 af[2][4];
#pragma unroll
    for (int r = 0; r < 4; ++r) { ag[r] = f32x4{0, 0, 0, 0}; au[r] = f32x4{0, 0, 0, 0}; }
    if (H == 0) { if (STATS) WAIT_V(3); bar_raw(); STAMP(6); }
#pragma unroll
    for (int r = 0; r < 4; ++r) af[0][r] = ldsr128(lds, a3_off(H, 0) + r * 1024 + v16);
#pragma unroll
    for (int ks = 0; ks < 20; ++ks) {
      if (H == 0) {
        if (ks == 3) { WAIT_V(3); bar_raw(); }
        if (ks == 9) { WAIT_V(4); bar_raw(); }
        if (STATS && ks >= 13) {
#pragma unroll
          for (int i = 5 * (ks - 13); i < 5 * (ks - 12); ++i) bd[i] = __builtin_amdgcn_raw_buffer_load_b128(rws, (i < 30 ? v16 : v7) + (i & 3) * 1024, sod + (i >> 2) * 4096, 0);
        }
      } else if (ks == 7) {
        if (STATS) WAIT_V(0); else WAIT_V(14);
        bar_raw(); STAMP(3);
        if (STATS) signal_flag(flags + 64, lane);
      } else if (ks == 17) {
        if (STATS) wait_flag(flags + 64);
        bar_raw(); STAMP(4);
      }
      if (ks < 19) {
#pragma unroll
        for (int r = 0; r < 4; ++r) af[(ks + 1) & 1][r] = ldsr128(lds, a3_off(H, ks + 1) + r * 1024 + v16);
      }
      SCHED();
#pragma unroll
      for (int r = 0; r < 4; ++r) {
        if (STATS) { ag[r] = mfma(ones, af[ks & 1][r], ag[r]); au[r] = mfma(af[ks & 1][r], af[ks & 1][r], au[r]); }
        else { ag[r] = mfma(bg[ks], af[ks & 1][r], ag[r]); au[r] = mfma(bu[ks], af[ks & 1][r], au[r]); }
      }
      SCHED();
      if (H == 0 && ks < 5) dma16_sc1(ldsb + ((ks + 5) * 8 + w) * 1024, v16, srdWS, soh + kb[(ks + 5) >> 1] + ((ks + 5) & 1) * 2048);
      else if (H == 0 && ks < 13) dma16_sc1(ldsb + 81920 + ((ks - 5) * 8 + w) * 1024, v16, srdWS, soh + 81920 + kb[(ks - 5) >> 1] + ((ks - 5) & 1) * 2048);
      if (!STATS && H == 1) {
#pragma unroll
        for (int i = 2 * ks; i < 2 * ks + 2; ++i)
          if (i < 35) bd[i] = __builtin_amdgcn_raw_buffer_load_b128(rws, (i < 30 ? v16 : v7) + (i & 3) * 1024, sod + (i >> 2) * 4096, 0);
      }
      if (H == 1 && ks == 18) {
#pragma unroll
        for (int i = 0; i < 4; ++i) L[i] = __builtin_amdgcn_raw_buffer_load_b128(rws, v16 + i * 1024, soa, 16);
      }
      asm volatile("" ::: "memory");
    }
    if (STATS) {
      f32x2 sv[4];
#pragma unroll
      for (int r = 0; r < 4; ++r) {
        float mean = ag[r][0] * (1.0f / D_);
        float var = fmaxf(sel4(au[r], j & 3) * (1.0f / D_) - mean * mean, 0.0f);
        sv[r] = f32x2{mean, var + 1e-5f};
      }
      if (q == (j >> 2)) {
#pragma unroll
        for (int r = 0; r < 4; ++r) st[H * 64 + r * 16 + j] = sv[r];
      }
    }
    if (H == 0) { if (STATS) WAIT_V(35); else WAIT_V(0); }
    bar();
    STAMP(7 + H);
    if (H == 0) {
#pragma unroll
      for (int i = 8; i < 10; ++i) dma16_sc1(ldsb + ((i - 8) * 8 + w) * 1024, v16, srdWS, soh + 81920 + kb[4] + (i & 1) * 2048);
    }
    if (!STATS) {
#pragma unroll
      for (int p = 0; p < 2; ++p) {
        u32x4 o;
#pragma unroll
        for (int rr = 0; rr < 2; ++rr) {
          const int r = 2 * p + rr;
          f32x2 ms = st[H * 64 + r * 16 + j];
          const f32x4 t = ag[r] - ms[0] * csg, tu = au[r] - ms[0] * csu;
          f32x4 ex = t * (__builtin_amdgcn_rsqf(ms[1]) * -1.4426950408889634f);
#pragma unroll
          for (int e = 0; e < 4; ++e) ex[e] = __builtin_amdgcn_exp2f(ex[e]);
          ex = ex * ms[1] + ms[1];
#pragma unroll
          for (int e = 0; e < 4; ++e) ex[e] = __builtin_amdgcn_rcpf(ex[e]);
          const f32x4 a = (t * ex) * tu;
          o[2 * rr] = pk(a[0], a[1]); o[2 * rr + 1] = pk(a[2], a[3]);
        }
        __builtin_amdgcn_raw_buffer_store_b128(o, rws, vB3 + WS_A + ((g * 4 + H * 2 + p) * 108 + ff) * 1024, 0, 16);
      }
      asm volatile("" ::: "memory");
      SCHED();
    }
  }
#pragma unroll
  for (int i = 4; i < 14; ++i) L[i] = __builtin_amdgcn_raw_buffer_load_b128(rws, (i < 12 ? v16 : v7) + (i & 3) * 1024, soa + (i >> 2) * 4096, 16);
  SCHED();
#pragma unroll
  for (int H = 0; H < 2; ++H) {
    f32x4 acc[2][5];
#pragma unroll
    for (int c = 0; c < 5; ++c) { acc[0][c] = f32x4{0, 0, 0, 0}; acc[1][c] = f32x4{0, 0, 0, 0}; }
#pragma unroll
    for (int t = 0; t < 7; ++t) {
      const u32x4 r0 = {L[2 * t][0], L[2 * t][1], L[2 * t + 1][0], L[2 * t + 1][1]}, r1 = {L[2 * t][2], L[2 * t][3], L[2 * t + 1][2], L[2 * t + 1][3]};
#pragma unroll
      for (int c = 0; c < 5; ++c) { acc[0][c] = mfma(bd[t * 5 + c], r0, acc[0][c]); acc[1][c] = mfma(bd[t * 5 + c], r1, acc[1][c]); }
      if (H == 0 && t == 1) {
        SCHED(); if (!STATS) WAIT_V(10); bar_raw(); STAMP(9);
        if (STATS) signal_flag(flags + 128, lane);
      }
    }
    asm volatile("" : "+v"(acc[0][0]), "+v"(acc[0][1]), "+v"(acc[0][2]), "+v"(acc[0][3]), "+v"(acc[0][4]), "+v"(acc[1][0]), "+v"(acc[1][1]),
                 "+v"(acc[1][2]), "+v"(acc[1][3]), "+v"(acc[1][4])::"memory");
    if (H == 1) bar();
    STAMP(10 + 2 * H);
#pragma unroll
    for (int r = 0; r < 2; ++r)
#pragma unroll
      for (int c = 0; c < 5; ++c) *(LAS f32x4*)(lds + w * 10752 + (16 * r + j) * K2_PSTR + (16 * c + 4 * q) * 4) = acc[r][c];
    if (H == 0 && STATS) wait_flag(flags + 128);
    bar();
    if (H == 0) {
#pragma unroll
      for (int i = 0; i < 14; ++i) L[i] = __builtin_amdgcn_raw_buffer_load_b128(rws, (i < 12 ? v16 : v7) + (i & 3) * 1024, soa + 2 * 108 * 1024 + (i >> 2) * 4096, 16);
      SCHED();
    }
    if (!STATS && tid < 320) {
      const int row = tid / 10, oct = tid % 10;
      const unsigned pa = row * K2_PSTR + oct * 32;
      f32x4 lo = {0, 0, 0, 0}, hi = {0, 0, 0, 0};
#pragma unroll
      for (int k = 0; k < 8; ++k) { lo += *(LAS f32x4*)(lds + pa + k * 10752); hi += *(LAS f32x4*)(lds + pa + k * 10752 + 16); }
      const u32x4 h = ldsr128(lds, K2_HST + (((2 * H + (row >> 4)) * 10 + oct) * 16 + (row & 15)) * 16);
      u32x4 o = {pk(lo[0] + bflo(h[0]), lo[1] + bfhi(h[0])), pk(lo[2] + bflo(h[1]), lo[3] + bfhi(h[1])),
                 pk(hi[0] + bflo(h[2]), hi[1] + bfhi(h[2])), pk(hi[2] + bflo(h[3]), hi[3] + bfhi(h[3]))};
      __builtin_amdgcn_raw_buffer_store_b128(o, rout, (g * 128 + H * 64 + s * 32 + row) * 1280 + cg * 160 + oct * 16, 0, 0);
    }
    if (H == 0) STAMP(11);
  }
}

__global__ __launch_bounds__(512, 2) void k2_wo_ffn(const char* __restrict__ x, char* __restrict__ ws, char* __restrict__ out) {
  __shared__ __attribute__((aligned(1024))) char lds_[K2_LDS];
  LAS char* lds = (LAS char*)lds_;
  const unsigned ldsb = (unsigned)(unsigned long long)lds;
  const int tid = threadIdx.x, w = __builtin_amdgcn_readfirstlane(tid >> 6);
  int g, m;
  map_block(blockIdx.x, g, m);
  if (w == 7) k2_body<true>(lds, ldsb, w, tid, g, m, x, ws, out);
  else k2_body<false>(lds, ldsb, w, tid, g, m, x, ws, out);
}

extern "C" void kernel_launch(void* const* d_in, const int* in_sizes, int n_in, void* d_out, int out_size, void* d_ws, size_t ws_size,
                              hipStream_t stream) {
  const char* x = (const char*)d_in[0];
  k1_qkv_attn<<<256, 512, 0, stream>>>(x, (const char*)d_in[1], (const char*)d_in[2], (const char*)d_in[4], (const char*)d_in[5],
                                       (const char*)d_in[6], (char*)d_ws);
  k2_wo_ffn<<<256, 512, 0, stream>>>(x, (char*)d_ws, (char*)d_out);
}
```
